```python
import torch

def _softmax(x, axis=-1):
    return torch.softmax(x, dim=axis)
def _gelu(x):
    return torch.nn.functional.gelu(x, approximate="tanh")
def _silu(x):
    return torch.nn.functional.silu(x)
def _sigmoid(x):
    return torch.sigmoid(x)
def _layernorm(x, axis=-1, eps=1e-5):
    m = x.mean(dim=axis, keepdim=True)
    v = x.var(dim=axis, keepdim=True, unbiased=False)
    return (x - m) / torch.sqrt(v + eps)
def _scatter_add(tgt, idx, src, axis=0):
    return tgt.clone().index_add_(axis, idx, src)
def _segment_sum(src, seg, n):
    out = torch.zeros((n,) + src.shape[1:], dtype=src.dtype)
    return out.index_add_(0, seg, src)
def _pad(x, pw):
    flat = tuple(v for pair in reversed(pw) for v in pair)
    return torch.nn.functional.pad(x, flat)

kernel_name = 'standalone__dense_activation_silu__prompt'

def setup_inputs():

    _w = (lambda a: (lambda t: t.to(torch.bfloat16) if t.is_floating_point() else t)(torch.as_tensor(a)))
    B, S, D, F = 1, 2048, 640, 1728
    return {
        "x": _w((lambda *s: torch.randn(*s, dtype=torch.bfloat16) * 0.1)(B, S, D)),
        "w1": _w((lambda *s: torch.randn(*s, dtype=torch.bfloat16) * 3.2)(D, F) * (D ** -0.5)),
        "w2": _w((lambda *s: torch.randn(*s, dtype=torch.bfloat16) * 3.2)(F, D) * (F ** -0.5)),
    }

def reference(x, w1, w2):
    h = torch.einsum("bsd,df->bsf", x, w1)
    h = _silu(h)
    out = torch.einsum("bsf,fd->bsd", h, w2)
    return out
```

```cpp
#include <hip/hip_runtime.h>
#include <hip/hip_bf16.h>
#include <stdint.h>

typedef __attribute__((ext_vector_type(8))) short bf16x8;
typedef __attribute__((ext_vector_type(4))) short s16x4;
typedef __attribute__((ext_vector_type(4))) float f32x4;
typedef __attribute__((ext_vector_type(2))) unsigned u32x2;
typedef __attribute__((ext_vector_type(4))) unsigned u32x4;
#define LDS_AS __attribute__((address_space(3)))

constexpr int M_ = 2048, D_ = 640, F_ = 1728;
constexpr int NFB = F_ / 32;

#define WAIT_V(n) asm volatile("s_waitcnt vmcnt(" #n ")" ::: "memory")
#define BAR() __builtin_amdgcn_s_barrier()

__device__ __forceinline__ void glds16(const void* sbase, unsigned voff, unsigned lds) {
  asm volatile("s_mov_b32 m0, %2\n\ts_nop 0\n\tglobal_load_lds_dwordx4 %0, %1" ::"v"(voff), "s"(sbase), "s"(lds));
}
__device__ __forceinline__ void glds16_56(const void* sbase, unsigned voff, unsigned lds) {
  asm volatile(
      "s_mov_b32 m0, %2\n\ts_mov_b32 exec_hi, 0x00ffffff\n\tglobal_load_lds_dwordx4 %0, %1\n\ts_mov_b32 exec_hi, -1" ::"v"(voff),
      "s"(sbase), "s"(lds));
}
__device__ __forceinline__ unsigned lds_addr(const void* p) { return (unsigned)(uintptr_t)(LDS_AS const char*)p; }
__device__ __forceinline__ s16x4 tr_read(const char* p) {
  return __builtin_amdgcn_ds_read_tr16_b64_v4i16((LDS_AS s16x4*)p);
}
__device__ __forceinline__ bf16x8 cat(s16x4 lo, s16x4 hi) {
  return __builtin_shufflevector(lo, hi, 0, 1, 2, 3, 4, 5, 6, 7);
}
__device__ __forceinline__ unsigned pack2(float a, float b) {
  __hip_bfloat16 x = __float2bfloat16(a), y = __float2bfloat16(b);
  return (unsigned)__builtin_bit_cast(unsigned short, x) | ((unsigned)__builtin_bit_cast(unsigned short, y) << 16);
}
__device__ __forceinline__ float bfround(float a) { return __bfloat162float(__float2bfloat16(a)); }
__device__ __forceinline__ float silu_f(float a) {
  float hb = bfround(a);
  return hb / (1.0f + __expf(-hb));
}

constexpr int G1_NS = 5;
constexpr int G1_XB = 128 * 128;
constexpr int G1_WB = 64 * 224;
constexpr int G1_SB = G1_XB + G1_WB;
constexpr int G1_LDS = G1_NS * G1_SB;

__global__ __launch_bounds__(256, 1) void ffn_gemm1(const __hip_bfloat16* __restrict__ x,
                                                    const __hip_bfloat16* __restrict__ w1,
                                                    char* __restrict__ hbuf) {
  extern __shared__ __attribute__((aligned(16))) char smem[];
  const int bid = blockIdx.x;
  const int xcd = bid & 7, idx = bid >> 3;
  const int mt = 4 * (xcd >> 1) + (idx >> 3);
  const int nt = 8 * (xcd & 1) + (idx & 7);
  const int m0 = mt * 128, n0 = nt * 112;
  const int tid = threadIdx.x;
  const int w = __builtin_amdgcn_readfirstlane(tid >> 6);
  const int lane = tid & 63;
  const int g = lane >> 4, c = lane & 15;

  const unsigned lbase = lds_addr(smem);
  unsigned xsrc[4];
#pragma unroll
  for (int q = 0; q < 4; ++q) {
    int row = 32 * w + 8 * q + (lane >> 3);
    int ch = (lane & 7) ^ ((row >> 1) & 7);
    xsrc[q] = ((m0 + row) * D_ + ch * 8) * 2;
  }
  unsigned wsrc[4];
  {
    int lr = lane / 14, ch = lane - lr * 14;
    int col = n0 + ch * 8;
    col = col > (F_ - 8) ? (F_ - 8) : col;
#pragma unroll
    for (int q = 0; q < 4; ++q) {
      int p = 16 * w + 4 * q + lr;
      int r = (p & 0x33) | ((p & 4) << 1) | ((p & 8) >> 1);
      wsrc[q] = (r * F_ + col) * 2;
    }
  }
  auto stage = [&](int t) {
    unsigned sb = lbase + (t % G1_NS) * G1_SB;
#pragma unroll
    for (int q = 0; q < 4; ++q) glds16((const char*)x + t * 128, xsrc[q], sb + (32 * w + 8 * q) * 128);
#pragma unroll
    for (int q = 0; q < 4; ++q)
      glds16_56((const char*)w1 + (size_t)t * 64 * F_ * 2, wsrc[q], sb + G1_XB + (16 * w + 4 * q) * 224);
  };

  const int xoff0 = (32 * w + c) * 128 + ((g ^ (c >> 1)) << 4);
  const int xoff1 = xoff0 ^ 64;
  const int woff = G1_XB + (16 * (g >> 1) + 4 * (g & 1) + (c >> 2)) * 224 + 8 * (c & 3);

  f32x4 acc[7][2];
#pragma unroll
  for (int n = 0; n < 7; ++n)
#pragma unroll
    for (int i = 0; i < 2; ++i) acc[n][i] = f32x4{0.f, 0.f, 0.f, 0.f};

  bf16x8 xa[2], wa[7], xb[2], wb[7];
  auto ldfrag = [&](int t, int s, bf16x8(&xf)[2], bf16x8(&wf)[7]) {
    const char* sb = smem + (t % G1_NS) * G1_SB;
#pragma unroll
    for (int i = 0; i < 2; ++i) xf[i] = *(const bf16x8*)(sb + (s ? xoff1 : xoff0) + i * 2048);
#pragma unroll
    for (int n = 0; n < 7; ++n) {
      s16x4 lo = tr_read(sb + woff + s * 7168 + n * 32);
      s16x4 hi = tr_read(sb + woff + s * 7168 + 1792 + n * 32);
      wf[n] = cat(lo, hi);
    }
  };
  auto mma = [&](bf16x8(&xf)[2], bf16x8(&wf)[7]) {
#pragma unroll
    for (int n = 0; n < 7; ++n)
#pragma unroll
      for (int i = 0; i < 2; ++i)
        acc[n][i] = __builtin_amdgcn_mfma_f32_16x16x32_bf16(wf[n], xf[i], acc[n][i], 0, 0, 0);
  };

#pragma unroll
  for (int t = 0; t < G1_NS; ++t) stage(t);

#pragma unroll
  for (int t = 0; t < 10; ++t) {
    if (t == 0) { WAIT_V(24); }
    else if (t <= 6) { WAIT_V(16); }
    else if (t == 7) { WAIT_V(8); }
    else if (t == 8) { WAIT_V(0); }
    if (t <= 8) BAR();
    if (t == 0) ldfrag(0, 0, xa, wa);
    if (t >= 1 && t + 4 <= 9) stage(t + 4);
    ldfrag(t, 1, xb, wb);
    mma(xa, wa);
    if (t < 9) ldfrag(t + 1, 0, xa, wa);
    mma(xb, wb);
  }

#pragma unroll
  for (int i = 0; i < 2; ++i) {
    const int m16 = 8 * mt + 2 * w + i;
#pragma unroll
    for (int n = 0; n < 7; ++n) {
      const int T = 7 * nt + n;
      if (T < F_ / 16) {
        u32x2 v;
        v[0] = pack2(silu_f(acc[n][i][0]), silu_f(acc[n][i][1]));
        v[1] = pack2(silu_f(acc[n][i][2]), silu_f(acc[n][i][3]));
        char* dst = hbuf + ((size_t)(m16 * NFB + (T >> 1)) * 64 + lane) * 16 + (T & 1) * 8;
        *(u32x2*)dst = v;
      }
    }
  }
}

constexpr int G2_R = 4;
constexpr int G2_SB = 4096 + 5120;
constexpr int G2_WB = G2_R * G2_SB;
constexpr int G2_LDS = 4 * G2_WB;
constexpr int G2_RS = 336;

template <int NSTEP>
__device__ __forceinline__ void g2_body(char* wl, const char* hbase, const char* wbase, const unsigned (&wsrc)[5],
                                        int lane, f32x4 (&acc)[5][4]) {
  const int g = lane >> 4, c = lane & 15;
  const unsigned hoff = lane * 16;
  const int woff = 4096 + (4 * g + (c >> 2)) * 160 + 8 * (c & 3);
  auto stage = [&](int s) {
    unsigned sb = lds_addr(wl) + (s % G2_R) * G2_SB;
#pragma unroll
    for (int i = 0; i < 4; ++i) glds16(hbase + (size_t)i * NFB * 1024 + s * 1024, hoff, sb + i * 1024);
#pragma unroll
    for (int q = 0; q < 5; ++q) glds16(wbase + (size_t)s * 32 * D_ * 2, wsrc[q], sb + 4096 + q * 1024);
  };
  bf16x8 ha[4], wa[5], hb[4], wb[5];
  auto ldfrag = [&](int s, bf16x8(&hf)[4], bf16x8(&wf)[5]) {
    const char* sb = wl + (s % G2_R) * G2_SB;
#pragma unroll
    for (int i = 0; i < 4; ++i) hf[i] = *(const bf16x8*)(sb + hoff + i * 1024);
#pragma unroll
    for (int t = 0; t < 5; ++t) {
      s16x4 lo = tr_read(sb + woff + t * 32);
      s16x4 hi = tr_read(sb + woff + 2560 + t * 32);
      wf[t] = cat(lo, hi);
    }
  };
  auto mma = [&](bf16x8(&hf)[4], bf16x8(&wf)[5]) {
#pragma unroll
    for (int t = 0; t < 5; ++t)
#pragma unroll
      for (int i = 0; i < 4; ++i)
        acc[t][i] = __builtin_amdgcn_mfma_f32_16x16x32_bf16(wf[t], hf[i], acc[t][i], 0, 0, 0);
  };
#pragma unroll
  for (int s = 0; s < G2_R; ++s) stage(s);
  WAIT_V(27);
  ldfrag(0, ha, wa);
#pragma unroll
  for (int s = 0; s < NSTEP; ++s) {
    if (s + 1 < NSTEP) {
      if (s + 3 <= NSTEP - 1) { WAIT_V(18); }
      else if (s + 2 <= NSTEP - 1) { WAIT_V(9); }
      else { WAIT_V(0); }
      if (s & 1) ldfrag(s + 1, ha, wa); else ldfrag(s + 1, hb, wb);
    }
    if (s + 4 < NSTEP) stage(s + 4);
    if (s & 1) mma(hb, wb); else mma(ha, wa);
  }
}

__global__ __launch_bounds__(256, 1) void ffn_gemm2(const char* __restrict__ hbuf,
                                                    const __hip_bfloat16* __restrict__ w2,
                                                    __hip_bfloat16* __restrict__ out) {
  extern __shared__ __attribute__((aligned(16))) char smem[];
  const int bid = blockIdx.x;
  const int xcd = bid & 7, idx = bid >> 3;
  const int mt = 8 * (xcd >> 1) + (idx >> 2);
  const int nt = 4 * (xcd & 1) + (idx & 3);
  const int n0 = nt * 80;
  const int tid = threadIdx.x;
  const int w = __builtin_amdgcn_readfirstlane(tid >> 6);
  const int lane = tid & 63;
  const int g = lane >> 4, c = lane & 15;
  const int kb = (w < 2) ? 14 * w : 28 + 13 * (w - 2);

  char* wl = smem + w * G2_WB;
  const char* hbase = hbuf + (size_t)(4 * mt * NFB + kb) * 1024;
  const char* wbase = (const char*)(w2 + (size_t)(32 * kb) * D_ + n0);
  unsigned wsrc[5];
#pragma unroll
  for (int q = 0; q < 5; ++q) {
    int o = 1024 * q + 16 * lane;
    int row = o / 160, ch = (o - row * 160) >> 4;
    wsrc[q] = (row * D_ + ch * 8) * 2;
  }
  f32x4 acc[5][4];
#pragma unroll
  for (int t = 0; t < 5; ++t)
#pragma unroll
    for (int i = 0; i < 4; ++i) acc[t][i] = f32x4{0.f, 0.f, 0.f, 0.f};

  if (w < 2) g2_body<14>(wl, hbase, wbase, wsrc, lane, acc);
  else g2_body<13>(wl, hbase, wbase, wsrc, lane, acc);

#pragma unroll
  for (int t = 0; t < 5; ++t)
#pragma unroll
    for (int i = 0; i < 4; ++i)
      *(f32x4*)(wl + (16 * i + c) * G2_RS + (16 * t + 4 * g) * 4) = acc[t][i];
  __syncthreads();
#pragma unroll
  for (int r = 0; r < 5; ++r) {
    int id = tid + 256 * r;
    int row = id / 20, p = id - row * 20;
    const char* src = smem + row * G2_RS + p * 16;
    f32x4 s0 = *(const f32x4*)(src);
    f32x4 s1 = *(const f32x4*)(src + G2_WB);
    f32x4 s2 = *(const f32x4*)(src + 2 * G2_WB);
    f32x4 s3 = *(const f32x4*)(src + 3 * G2_WB);
    f32x4 s = (s0 + s1) + (s2 + s3);
    u32x2 v;
    v[0] = pack2(s[0], s[1]);
    v[1] = pack2(s[2], s[3]);
    *(u32x2*)(out + (size_t)(64 * mt + row) * D_ + n0 + p * 4) = v;
  }
}

extern "C" void kernel_launch(void* const* d_in, const int* in_sizes, int n_in, void* d_out, int out_size,
                              void* d_ws, size_t ws_size, hipStream_t stream) {
  const __hip_bfloat16* x = (const __hip_bfloat16*)d_in[0];
  const __hip_bfloat16* w1 = (const __hip_bfloat16*)d_in[1];
  const __hip_bfloat16* w2 = (const __hip_bfloat16*)d_in[2];
  __hip_bfloat16* out = (__hip_bfloat16*)d_out;
  char* hbuf = (char*)d_ws;
  (void)hipFuncSetAttribute((const void*)ffn_gemm1, hipFuncAttributeMaxDynamicSharedMemorySize, G1_LDS);
  (void)hipFuncSetAttribute((const void*)ffn_gemm2, hipFuncAttributeMaxDynamicSharedMemorySize, G2_LDS);
  ffn_gemm1<<<256, 256, G1_LDS, stream>>>(x, w1, hbuf);
  ffn_gemm2<<<256, 256, G2_LDS, stream>>>(hbuf, w2, out);
}
```

```cpp
#include <hip/hip_runtime.h>
#include <hip/hip_bf16.h>
#include <stdint.h>

typedef __attribute__((ext_vector_type(8))) short bf16x8;
typedef __attribute__((ext_vector_type(4))) short s16x4;
typedef __attribute__((ext_vector_type(4))) float f32x4;
typedef __attribute__((ext_vector_type(2))) unsigned u32x2;
typedef __attribute__((ext_vector_type(4))) unsigned u32x4;
#define LDS_AS __attribute__((address_space(3)))

constexpr int M_ = 2048, D_ = 640, F_ = 1728;
constexpr int NFB = F_ / 32;
constexpr size_t H_BYTES = (size_t)M_ * F_ * 2;
constexpr size_t W2P_OFF = 8u << 20;

#define DBG_ARG
#define STAMP(k)
#define RSTAMP(k)
#define STAMP_END()
#define WAIT_V(n) asm volatile("s_waitcnt vmcnt(%0)" ::"i"(n) : "memory")
#define BAR() __builtin_amdgcn_s_barrier()
#define SGB(mask, n) __builtin_amdgcn_sched_group_barrier(mask, n, 0)
#define SCHED_FENCE() __builtin_amdgcn_sched_barrier(0)

__device__ __forceinline__ void glds16(const void* sbase, unsigned voff, unsigned lds) {
  asm volatile("s_mov_b32 m0, %2\n\ts_nop 0\n\tglobal_load_lds_dwordx4 %0, %1" ::"v"(voff), "s"(sbase), "s"(lds));
}
__device__ __forceinline__ unsigned lds_addr(const void* p) { return (unsigned)(uintptr_t)(LDS_AS const char*)p; }
__device__ __forceinline__ s16x4 tr_read(const char* p) {
  return __builtin_amdgcn_ds_read_tr16_b64_v4i16((LDS_AS s16x4*)p);
}
__device__ __forceinline__ bf16x8 cat(s16x4 lo, s16x4 hi) {
  return __builtin_shufflevector(lo, hi, 0, 1, 2, 3, 4, 5, 6, 7);
}
__device__ __forceinline__ unsigned pack2(float a, float b) {
  __hip_bfloat16 x = __float2bfloat16(a), y = __float2bfloat16(b);
  return (unsigned)__builtin_bit_cast(unsigned short, x) | ((unsigned)__builtin_bit_cast(unsigned short, y) << 16);
}
__device__ __forceinline__ unsigned silu2(float a, float b) {
  unsigned p = pack2(a, b);
  float ha = __builtin_bit_cast(float, p << 16), hb = __builtin_bit_cast(float, p & 0xffff0000u);
  float ea = __builtin_amdgcn_exp2f(ha * -1.4426950408889634f), eb = __builtin_amdgcn_exp2f(hb * -1.4426950408889634f);
  float ra = __builtin_amdgcn_rcpf(1.0f + ea), rb = __builtin_amdgcn_rcpf(1.0f + eb);
  return pack2(ha * ra, hb * rb);
}

#define NL 4
constexpr int NTHR = 512 + 64 * NL;
constexpr int G1_NS = 4;
constexpr int G1_XB = 128 * 128;
constexpr int G1_WB = 64 * 256;
constexpr int G1_SB = G1_XB + G1_WB;
constexpr int G1_LDS = G1_NS * G1_SB;
constexpr int G1_NT = D_ / 64;

__global__ __launch_bounds__(NTHR, NTHR / 256) void ffn_gemm1(const __hip_bfloat16* __restrict__ x,
                                                    const __hip_bfloat16* __restrict__ w1,
                                                    const __hip_bfloat16* __restrict__ w2,
                                                    char* __restrict__ ws DBG_ARG) {
  extern __shared__ __attribute__((aligned(16))) char smem[];
  const int bid = blockIdx.x;
  const int xcd = bid & 7, idx = bid >> 3;
  const int tid = threadIdx.x;

  if (idx >= 28) {
    if (tid >= 512) return;
    const int gt = ((idx - 28) * 8 + xcd) * 512 + tid;
    char* dst = ws + W2P_OFF;
    constexpr int NCH = F_ * D_ / 8;
    u32x4 v[9];
#pragma unroll
    for (int r = 0; r < 9; ++r) {
      int ch = gt + r * 16384;
      if (ch < NCH) {
        int blk = ch / 320, o = ch - blk * 320;
        int nt = blk / NFB, b = blk - nt * NFB;
        int row = o / 10, cc = o - row * 10;
        v[r] = *(const u32x4*)(w2 + (size_t)(32 * b + row) * D_ + nt * 80 + cc * 8);
      }
    }
#pragma unroll
    for (int r = 0; r < 9; ++r) {
      int ch = gt + r * 16384;
      if (ch < NCH) *(u32x4*)(dst + (size_t)ch * 16) = v[r];
    }
    return;
  }

  const int mt = 4 * (xcd >> 1) + idx / 7;
  const int nt = 7 * (xcd & 1) + idx % 7;
  const int m0 = mt * 128, n0 = nt * 128;
  const int w = __builtin_amdgcn_readfirstlane(tid >> 6);
  const int lane = tid & 63;
  STAMP(0); RSTAMP(14);
  if (w >= 8) {
    const int l = w - 8;
    constexpr int PQ = 16 / NL;
    const unsigned lbase = lds_addr(smem);
    unsigned xsrc[PQ], wsrc[PQ];
#pragma unroll
    for (int q = 0; q < PQ; ++q) {
      int row = 8 * (PQ * l + q) + (lane >> 3);
      int ch = (lane & 7) ^ ((row >> 1) & 7);
      xsrc[q] = ((m0 + row) * D_ + ch * 8) * 2;
      int p = 4 * (PQ * l + q) + (lane >> 4), pos = lane & 15;
      int r = (p & 0x33) | ((p & 4) << 1) | ((p & 8) >> 1);
      int wch = (((pos >> 1) ^ (p & 7)) << 1) | (pos & 1);
      int col = n0 + wch * 8;
      col = col > (F_ - 8) ? (F_ - 8) : col;
      wsrc[q] = (r * F_ + col) * 2;
    }
    auto stage = [&](int t, unsigned sb) {
#pragma unroll
      for (int q = 0; q < PQ; ++q) {
        glds16((const char*)x + t * 128, xsrc[q], sb + (PQ * l + q) * 1024);
        glds16((const char*)w1 + (size_t)t * 64 * F_ * 2, wsrc[q], sb + G1_XB + (PQ * l + q) * 1024);
      }
    };
#pragma unroll
    for (int t = 0; t < G1_NS - 1; ++t) stage(t, lbase + t * G1_SB);
    STAMP(1);
    int ps = G1_NS - 1;
#pragma unroll 1
    for (int t = 0; t < G1_NT - (G1_NS - 1); ++t) {
      WAIT_V(2 * PQ * (G1_NS - 3));
      BAR();
      stage(t + G1_NS - 1, lbase + ps * G1_SB);
      ps = ps == G1_NS - 1 ? 0 : ps + 1;
    }
#pragma unroll
    for (int r = G1_NS - 3; r >= -1; --r) {
      WAIT_V(2 * PQ * (r < 0 ? 0 : r));
      BAR();
    }
    return;
  }

  const int wm = w >> 1, wn = w & 1;
  const int g = lane >> 4, c = lane & 15;
  const int xoff0 = (32 * wm + c) * 128 + ((g ^ (c >> 1)) << 4);
  const int xoff1 = xoff0 ^ 64;
  int woff[4];
  {
    int p = 16 * (g >> 1) + 4 * (g & 1) + (c >> 2);
#pragma unroll
    for (int n = 0; n < 4; ++n) woff[n] = G1_XB + p * 256 + (((4 * wn + n) ^ (p & 7)) << 5) + 8 * (c & 3);
  }

  f32x4 acc[4][2];
#pragma unroll
  for (int n = 0; n < 4; ++n)
#pragma unroll
    for (int i = 0; i < 2; ++i) acc[n][i] = f32x4{0.f, 0.f, 0.f, 0.f};

  bf16x8 xa[2], wa[4], xb[2], wb[4];
  auto ldfrag = [&](const char* sb, int s, bf16x8(&xf)[2], bf16x8(&wf)[4]) {
#pragma unroll
    for (int i = 0; i < 2; ++i) xf[i] = *(const bf16x8*)(sb + (s ? xoff1 : xoff0) + i * 2048);
#pragma unroll
    for (int n = 0; n < 4; ++n)
      wf[n] = cat(tr_read(sb + woff[n] + s * 8192), tr_read(sb + woff[n] + s * 8192 + 2048));
  };
  auto mma = [&](bf16x8(&xf)[2], bf16x8(&wf)[4]) {
#pragma unroll
    for (int n = 0; n < 4; ++n)
#pragma unroll
      for (int i = 0; i < 2; ++i)
        acc[n][i] = __builtin_amdgcn_mfma_f32_16x16x32_bf16(wf[n], xf[i], acc[n][i], 0, 0, 0);
  };
  auto interleave = [&]() {
    SGB(0x100, 2);
#pragma unroll
    for (int j = 0; j < 8; ++j) { SGB(0x8, 1); SGB(0x100, 1); }
  };
  BAR();
  STAMP(2);
  ldfrag(smem, 0, xa, wa);
  SCHED_FENCE();
  int slot = 0;
#pragma unroll 1
  for (int t = 0; t < G1_NT - 1; ++t) {
    const char* sb = smem + slot * G1_SB;
    slot = slot == G1_NS - 1 ? 0 : slot + 1;
    ldfrag(sb, 1, xb, wb);
    mma(xa, wa);
    interleave();
    SCHED_FENCE();
    ldfrag(smem + slot * G1_SB, 0, xa, wa);
    mma(xb, wb);
    interleave();
    SCHED_FENCE();
    BAR();
    STAMP(3 + t);
  }
  ldfrag(smem + slot * G1_SB, 1, xb, wb);
  mma(xa, wa);
  interleave();
  SCHED_FENCE();
  mma(xb, wb);

  STAMP(12);
#pragma unroll
  for (int j = 0; j < 2; ++j) {
    const int fb = 4 * nt + 2 * wn + j;
    if (fb < NFB) {
#pragma unroll
      for (int i = 0; i < 2; ++i) {
        const int m16 = 8 * mt + 2 * wm + i;
        u32x4 v;
        v[0] = silu2(acc[2 * j][i][0], acc[2 * j][i][1]);
        v[1] = silu2(acc[2 * j][i][2], acc[2 * j][i][3]);
        v[2] = silu2(acc[2 * j + 1][i][0], acc[2 * j + 1][i][1]);
        v[3] = silu2(acc[2 * j + 1][i][2], acc[2 * j + 1][i][3]);
        *(u32x4*)(ws + ((size_t)(m16 * NFB + fb) * 64 + lane) * 16) = v;
      }
    }
  }
  STAMP_END();
}

constexpr int G2_NS = 8;
constexpr int G2_SB = 8192 + 10240;
constexpr int G2_LDS = G2_NS * G2_SB + 64;
constexpr int G2_NT = NFB / 2;

__global__ __launch_bounds__(NTHR, NTHR / 256) void ffn_gemm2(const char* __restrict__ ws, __hip_bfloat16* __restrict__ out DBG_ARG) {
  extern __shared__ __attribute__((aligned(16))) char smem[];
  const int bid = blockIdx.x;
  const int xcd = bid & 7, idx = bid >> 3;
  const int mt = 8 * (xcd >> 1) + (idx >> 2);
  const int nt = 4 * (xcd & 1) + (idx & 3);
  const int tid = threadIdx.x;
  const int w = __builtin_amdgcn_readfirstlane(tid >> 6);
  const int lane = tid & 63;
  STAMP(0); RSTAMP(14);
  if (w >= 8) {
    const int l = w - 8;
    const unsigned lbase = lds_addr(smem);
    const unsigned loff = lane * 16;
    const char* hbase = ws + (size_t)((4 * mt + 2 * (l & 1)) * NFB + (l >> 1)) * 1024;
    const char* wbase = ws + W2P_OFF + (size_t)nt * NFB * 5120 + l * 1024;
    const unsigned hl = (l >> 1) * 4096 + (l & 1) * 2048;
    constexpr int CH = 5, CL = 4;
    auto stage = [&](int t, unsigned sb) {
      glds16(hbase + t * 2048, loff, sb + hl);
      glds16(wbase + t * 10240, loff, sb + 8192 + l * 1024);
      glds16(hbase + t * 2048 + NFB * 1024, loff, sb + hl + 1024);
      glds16(wbase + t * 10240 + 4096, loff, sb + 8192 + 4096 + l * 1024);
      if (l < 2) glds16(wbase + t * 10240 + 8192, loff, sb + 8192 + 8192 + l * 1024);
    };
#pragma unroll
    for (int t = 0; t < G2_NS - 1; ++t) stage(t, lbase + t * G2_SB);
    STAMP(1);
    int ps = G2_NS - 1;
#pragma unroll 1
    for (int t = 0; t < G2_NT - (G2_NS - 1); ++t) {
      if (l < 2) { WAIT_V(CH * (G2_NS - 3)); } else { WAIT_V(CL * (G2_NS - 3)); }
      BAR();
      stage(t + G2_NS - 1, lbase + ps * G2_SB);
      ps = ps == G2_NS - 1 ? 0 : ps + 1;
    }
#pragma unroll
    for (int r = G2_NS - 3; r >= -1; --r) {
      if (l < 2) { WAIT_V(CH * (r < 0 ? 0 : r)); } else { WAIT_V(CL * (r < 0 ? 0 : r)); }
      BAR();
    }
    BAR();
    BAR();
    return;
  }

  const int kg = w >> 2, mh = (w >> 1) & 1, nh = (w & 1) ^ kg;
  const int g = lane >> 4, c = lane & 15;
  const int hoff = kg * 4096 + mh * 2048 + lane * 16;
  const int woff = 8192 + kg * 5120 + (4 * g + (c >> 2)) * 160 + 8 * (c & 3) + nh * 96;
  f32x4 acc[3][2];
#pragma unroll
  for (int t = 0; t < 3; ++t)
#pragma unroll
    for (int i = 0; i < 2; ++i) acc[t][i] = f32x4{0.f, 0.f, 0.f, 0.f};

  bf16x8 ha[2], wa[3], hb[2], wb[3];
  auto ldfrag = [&](const char* sb, bf16x8(&hf)[2], bf16x8(&wf)[3]) {
#pragma unroll
    for (int i = 0; i < 2; ++i) hf[i] = *(const bf16x8*)(sb + hoff + i * 1024);
#pragma unroll
    for (int q = 0; q < 3; ++q) wf[q] = cat(tr_read(sb + woff + q * 32), tr_read(sb + woff + 2560 + q * 32));
  };
  auto mma = [&](bf16x8(&hf)[2], bf16x8(&wf)[3]) {
#pragma unroll
    for (int q = 0; q < 2; ++q)
#pragma unroll
      for (int i = 0; i < 2; ++i)
        acc[q][i] = __builtin_amdgcn_mfma_f32_16x16x32_bf16(wf[q], hf[i], acc[q][i], 0, 0, 0);
    if (nh == 0) {
#pragma unroll
      for (int i = 0; i < 2; ++i)
        acc[2][i] = __builtin_amdgcn_mfma_f32_16x16x32_bf16(wf[2], hf[i], acc[2][i], 0, 0, 0);
    }
  };
  BAR();
  STAMP(2);
  ldfrag(smem, ha, wa);
  SCHED_FENCE();
  int slot = 0;
#pragma unroll 1
  for (int t = 0; t < G2_NT - 1; t += 2) {
    slot = slot == G2_NS - 1 ? 0 : slot + 1;
    ldfrag(smem + slot * G2_SB, hb, wb);
    SCHED_FENCE();
    mma(ha, wa);
    SCHED_FENCE();
    BAR();
    slot = slot == G2_NS - 1 ? 0 : slot + 1;
    ldfrag(smem + slot * G2_SB, ha, wa);
    SCHED_FENCE();
    mma(hb, wb);
    SCHED_FENCE();
    BAR();
    if (t < 18) STAMP(3 + t / 2);
  }
  mma(ha, wa);

  STAMP(12);
  BAR();
  char* rp = smem + (((mh << 1) | nh) * 6) * 1024 + lane * 16;
  if (kg == 1) {
#pragma unroll
    for (int t = 0; t < 3; ++t)
#pragma unroll
      for (int i = 0; i < 2; ++i)
        if (t < 2 || nh == 0) *(f32x4*)(rp + (t * 2 + i) * 1024) = acc[t][i];
  }
  asm volatile("s_waitcnt lgkmcnt(0)" ::: "memory");
  BAR();
  if (kg == 0) {
#pragma unroll
    for (int t = 0; t < 3; ++t)
#pragma unroll
      for (int i = 0; i < 2; ++i)
        if (t < 2 || nh == 0) {
          f32x4 s = acc[t][i] + *(const f32x4*)(rp + (t * 2 + i) * 1024);
          u32x2 v;
          v[0] = pack2(s[0], s[1]);
          v[1] = pack2(s[2], s[3]);
          *(u32x2*)(out + (size_t)(64 * mt + 32 * mh + 16 * i + c) * D_ + nt * 80 + nh * 48 + 16 * t + 4 * g) = v;
        }
  }
  STAMP_END();
}

extern "C" void kernel_launch(void* const* d_in, const int* in_sizes, int n_in, void* d_out, int out_size,
                              void* d_ws, size_t ws_size, hipStream_t stream) {
  const __hip_bfloat16* x = (const __hip_bfloat16*)d_in[0];
  const __hip_bfloat16* w1 = (const __hip_bfloat16*)d_in[1];
  const __hip_bfloat16* w2 = (const __hip_bfloat16*)d_in[2];
  __hip_bfloat16* out = (__hip_bfloat16*)d_out;
  char* ws = (char*)d_ws;
  (void)hipFuncSetAttribute((const void*)ffn_gemm1, hipFuncAttributeMaxDynamicSharedMemorySize, G1_LDS);
  (void)hipFuncSetAttribute((const void*)ffn_gemm2, hipFuncAttributeMaxDynamicSharedMemorySize, G2_LDS);
  ffn_gemm1<<<256, NTHR, G1_LDS, stream>>>(x, w1, w2, ws);
  ffn_gemm2<<<256, NTHR, G2_LDS, stream>>>(ws, out);
}
```

```cpp
#include <hip/hip_runtime.h>
#include <hip/hip_bf16.h>
#include <stdint.h>

typedef __attribute__((ext_vector_type(8))) short bf16x8;
typedef __attribute__((ext_vector_type(4))) short s16x4;
typedef __attribute__((ext_vector_type(4))) float f32x4;
typedef __attribute__((ext_vector_type(2))) unsigned u32x2;
typedef __attribute__((ext_vector_type(4))) unsigned u32x4;
#define LDS_AS __attribute__((address_space(3)))

constexpr int M_ = 2048, D_ = 640, F_ = 1728;
constexpr int NFB = F_ / 32;
constexpr size_t H_BYTES = (size_t)M_ * F_ * 2;
constexpr size_t W2P_OFF = 8u << 20;

#define DBG_ARG
#define STAMP(k)
#define RSTAMP(k)
#define STAMP_END()
#define WAIT_V(n) asm volatile("s_waitcnt vmcnt(%0)" ::"i"(n) : "memory")
#define BAR() __builtin_amdgcn_s_barrier()
#define SGB(mask, n) __builtin_amdgcn_sched_group_barrier(mask, n, 0)
#define SCHED_FENCE() __builtin_amdgcn_sched_barrier(0)

__device__ __forceinline__ void glds16(const void* sbase, unsigned voff, unsigned lds) {
  asm volatile("s_mov_b32 m0, %2\n\ts_nop 0\n\tglobal_load_lds_dwordx4 %0, %1" ::"v"(voff), "s"(sbase), "s"(lds));
}
#define ST_MODE 1
__device__ __forceinline__ void st16(void* p, u32x4 v) {
  asm volatile("global_store_dwordx4 %0, %1, off sc1\n\ts_nop 1" ::"v"(p), "v"(v) : "memory");
}
#define ST8_MODE 0
__device__ __forceinline__ void st8(void* p, u32x2 v) {
  *(u32x2*)p = v;
}
__device__ __forceinline__ unsigned lds_addr(const void* p) { return (unsigned)(uintptr_t)(LDS_AS const char*)p; }
__device__ __forceinline__ s16x4 tr_read(const char* p) {
  return __builtin_amdgcn_ds_read_tr16_b64_v4i16((LDS_AS s16x4*)p);
}
__device__ __forceinline__ bf16x8 cat(s16x4 lo, s16x4 hi) {
  return __builtin_shufflevector(lo, hi, 0, 1, 2, 3, 4, 5, 6, 7);
}
__device__ __forceinline__ unsigned pack2(float a, float b) {
  __hip_bfloat16 x = __float2bfloat16(a), y = __float2bfloat16(b);
  return (unsigned)__builtin_bit_cast(unsigned short, x) | ((unsigned)__builtin_bit_cast(unsigned short, y) << 16);
}
__device__ __forceinline__ unsigned silu2(float a, float b) {
  unsigned p = pack2(a, b);
  float ha = __builtin_bit_cast(float, p << 16), hb = __builtin_bit_cast(float, p & 0xffff0000u);
  float ea = __builtin_amdgcn_exp2f(ha * -1.4426950408889634f), eb = __builtin_amdgcn_exp2f(hb * -1.4426950408889634f);
  float ra = __builtin_amdgcn_rcpf(1.0f + ea), rb = __builtin_amdgcn_rcpf(1.0f + eb);
  return pack2(ha * ra, hb * rb);
}

#define NL 4
constexpr int NTHR = 512 + 64 * NL;
constexpr int G1_NS = 4;
constexpr int G1_XB = 128 * 128;
constexpr int G1_WB = 64 * 256;
constexpr int G1_SB = G1_XB + G1_WB;
constexpr int G1_LDS = G1_NS * G1_SB;
constexpr int G1_NT = D_ / 64;

__global__ __launch_bounds__(NTHR, NTHR / 256) void ffn_gemm1(const __hip_bfloat16* __restrict__ x,
                                                    const __hip_bfloat16* __restrict__ w1,
                                                    const __hip_bfloat16* __restrict__ w2,
                                                    char* __restrict__ ws DBG_ARG) {
  extern __shared__ __attribute__((aligned(16))) char smem[];
  const int bid = blockIdx.x;
  const int xcd = bid & 7, idx = bid >> 3;
  const int tid = threadIdx.x;

  if (idx >= 28) {
    if (tid >= 512) return;
    const int gt = ((idx - 28) * 8 + xcd) * 512 + tid;
    char* dst = ws + W2P_OFF;
    constexpr int NCH = F_ * D_ / 8;
    u32x4 v[9];
#pragma unroll
    for (int r = 0; r < 9; ++r) {
      int ch = gt + r * 16384;
      if (ch < NCH) {
        int blk = ch / 320, o = ch - blk * 320;
        int nt = blk / NFB, b = blk - nt * NFB;
        int row = o / 10, cc = o - row * 10;
        v[r] = *(const u32x4*)(w2 + (size_t)(32 * b + row) * D_ + nt * 80 + cc * 8);
      }
    }
#pragma unroll
    for (int r = 0; r < 9; ++r) {
      int ch = gt + r * 16384;
      if (ch < NCH) st16(dst + (size_t)ch * 16, v[r]);
    }
    STAMP_END();
    return;
  }

  const int mt = 4 * (xcd >> 1) + idx / 7;
  const int nt = 7 * (xcd & 1) + idx % 7;
  const int m0 = mt * 128, n0 = nt * 128;
  const int w = __builtin_amdgcn_readfirstlane(tid >> 6);
  const int lane = tid & 63;
  STAMP(0); RSTAMP(14);
  if (w >= 8) {
    const int l = w - 8;
    constexpr int PQ = 16 / NL;
    const unsigned lbase = lds_addr(smem);
    unsigned xsrc[PQ], wsrc[PQ];
#pragma unroll
    for (int q = 0; q < PQ; ++q) {
      int row = 8 * (PQ * l + q) + (lane >> 3);
      int ch = (lane & 7) ^ ((row >> 1) & 7);
      xsrc[q] = ((m0 + row) * D_ + ch * 8) * 2;
      int p = 4 * (PQ * l + q) + (lane >> 4), pos = lane & 15;
      int r = (p & 0x33) | ((p & 4) << 1) | ((p & 8) >> 1);
      int wch = (((pos >> 1) ^ (p & 7)) << 1) | (pos & 1);
      int col = n0 + wch * 8;
      col = col > (F_ - 8) ? (F_ - 8) : col;
      wsrc[q] = (r * F_ + col) * 2;
    }
    auto stage = [&](int t, unsigned sb) {
#pragma unroll
      for (int q = 0; q < PQ; ++q) {
        glds16((const char*)x + t * 128, xsrc[q], sb + (PQ * l + q) * 1024);
        glds16((const char*)w1 + (size_t)t * 64 * F_ * 2, wsrc[q], sb + G1_XB + (PQ * l + q) * 1024);
      }
    };
#pragma unroll
    for (int t = 0; t < G1_NS - 1; ++t) stage(t, lbase + t * G1_SB);
    STAMP(1);
    int ps = G1_NS - 1;
#pragma unroll 1
    for (int t = 0; t < G1_NT - (G1_NS - 1); ++t) {
      WAIT_V(2 * PQ * (G1_NS - 3));
      BAR();
      stage(t + G1_NS - 1, lbase + ps * G1_SB);
      ps = ps == G1_NS - 1 ? 0 : ps + 1;
    }
#pragma unroll
    for (int r = G1_NS - 3; r >= -1; --r) {
      WAIT_V(2 * PQ * (r < 0 ? 0 : r));
      BAR();
    }
    return;
  }

  const int wm = w >> 1, wn = w & 1;
  const int g = lane >> 4, c = lane & 15;
  const int xoff0 = (32 * wm + c) * 128 + ((g ^ (c >> 1)) << 4);
  const int xoff1 = xoff0 ^ 64;
  int woff[4];
  {
    int p = 16 * (g >> 1) + 4 * (g & 1) + (c >> 2);
#pragma unroll
    for (int n = 0; n < 4; ++n) woff[n] = G1_XB + p * 256 + (((4 * wn + n) ^ (p & 7)) << 5) + 8 * (c & 3);
  }

  f32x4 acc[4][2];
#pragma unroll
  for (int n = 0; n < 4; ++n)
#pragma unroll
    for (int i = 0; i < 2; ++i) acc[n][i] = f32x4{0.f, 0.f, 0.f, 0.f};

  bf16x8 xa[2], wa[4], xb[2], wb[4];
  auto ldfrag = [&](const char* sb, int s, bf16x8(&xf)[2], bf16x8(&wf)[4]) {
#pragma unroll
    for (int i = 0; i < 2; ++i) xf[i] = *(const bf16x8*)(sb + (s ? xoff1 : xoff0) + i * 2048);
#pragma unroll
    for (int n = 0; n < 4; ++n)
      wf[n] = cat(tr_read(sb + woff[n] + s * 8192), tr_read(sb + woff[n] + s * 8192 + 2048));
  };
  auto mma = [&](bf16x8(&xf)[2], bf16x8(&wf)[4]) {
#pragma unroll
    for (int n = 0; n < 4; ++n)
#pragma unroll
      for (int i = 0; i < 2; ++i)
        acc[n][i] = __builtin_amdgcn_mfma_f32_16x16x32_bf16(wf[n], xf[i], acc[n][i], 0, 0, 0);
  };
  auto interleave = [&]() {
    SGB(0x100, 2);
#pragma unroll
    for (int j = 0; j < 8; ++j) { SGB(0x8, 1); SGB(0x100, 1); }
  };
  BAR();
  STAMP(2);
  ldfrag(smem, 0, xa, wa);
  SCHED_FENCE();
  int slot = 0;
#pragma unroll 1
  for (int t = 0; t < G1_NT - 1; ++t) {
    const char* sb = smem + slot * G1_SB;
    slot = slot == G1_NS - 1 ? 0 : slot + 1;
    ldfrag(sb, 1, xb, wb);
    mma(xa, wa);
    interleave();
    SCHED_FENCE();
    ldfrag(smem + slot * G1_SB, 0, xa, wa);
    mma(xb, wb);
    interleave();
    SCHED_FENCE();
    BAR();
    STAMP(3 + t);
  }
  ldfrag(smem + slot * G1_SB, 1, xb, wb);
  mma(xa, wa);
  interleave();
  SCHED_FENCE();
  mma(xb, wb);

  STAMP(12);
#pragma unroll
  for (int j = 0; j < 2; ++j) {
    const int fb = 4 * nt + 2 * wn + j;
    if (fb < NFB) {
#pragma unroll
      for (int i = 0; i < 2; ++i) {
        const int m16 = 8 * mt + 2 * wm + i;
        u32x4 v;
        v[0] = silu2(acc[2 * j][i][0], acc[2 * j][i][1]);
        v[1] = silu2(acc[2 * j][i][2], acc[2 * j][i][3]);
        v[2] = silu2(acc[2 * j + 1][i][0], acc[2 * j + 1][i][1]);
        v[3] = silu2(acc[2 * j + 1][i][2], acc[2 * j + 1][i][3]);
        st16(ws + ((size_t)(m16 * NFB + fb) * 64 + lane) * 16, v);
      }
    }
  }
  STAMP_END();
}

constexpr int G2_NS = 8;
constexpr int G2_SB = 8192 + 10240;
constexpr int G2_LDS = G2_NS * G2_SB + 64;
constexpr int G2_NT = NFB / 2;

__global__ __launch_bounds__(NTHR, NTHR / 256) void ffn_gemm2(const char* __restrict__ ws, __hip_bfloat16* __restrict__ out DBG_ARG) {
  extern __shared__ __attribute__((aligned(16))) char smem[];
  const int bid = blockIdx.x;
  const int xcd = bid & 7, idx = bid >> 3;
  const int mt = 8 * (xcd >> 1) + (idx >> 2);
  const int nt = 4 * (xcd & 1) + (idx & 3);
  const int tid = threadIdx.x;
  const int w = __builtin_amdgcn_readfirstlane(tid >> 6);
  const int lane = tid & 63;
  STAMP(0); RSTAMP(14);
  if (w >= 8) {
    const int l = w - 8;
    const unsigned lbase = lds_addr(smem);
    const unsigned loff = lane * 16;
    const char* hbase = ws + (size_t)((4 * mt + 2 * (l & 1)) * NFB + (l >> 1)) * 1024;
    const char* wbase = ws + W2P_OFF + (size_t)nt * NFB * 5120 + l * 1024;
    const unsigned hl = (l >> 1) * 4096 + (l & 1) * 2048;
    constexpr int CH = 5, CL = 4;
    auto stage = [&](int t, unsigned sb) {
      glds16(hbase + t * 2048, loff, sb + hl);
      glds16(wbase + t * 10240, loff, sb + 8192 + l * 1024);
      glds16(hbase + t * 2048 + NFB * 1024, loff, sb + hl + 1024);
      glds16(wbase + t * 10240 + 4096, loff, sb + 8192 + 4096 + l * 1024);
      if (l < 2) glds16(wbase + t * 10240 + 8192, loff, sb + 8192 + 8192 + l * 1024);
    };
#pragma unroll
    for (int t = 0; t < G2_NS - 1; ++t) stage(t, lbase + t * G2_SB);
    STAMP(1);
    int ps = G2_NS - 1;
#pragma unroll 1
    for (int t = 0; t < G2_NT - (G2_NS - 1); ++t) {
      if (l < 2) { WAIT_V(CH * (G2_NS - 3)); } else { WAIT_V(CL * (G2_NS - 3)); }
      BAR();
      stage(t + G2_NS - 1, lbase + ps * G2_SB);
      ps = ps == G2_NS - 1 ? 0 : ps + 1;
    }
#pragma unroll
    for (int r = G2_NS - 3; r >= -1; --r) {
      if (l < 2) { WAIT_V(CH * (r < 0 ? 0 : r)); } else { WAIT_V(CL * (r < 0 ? 0 : r)); }
      BAR();
    }
    BAR();
    BAR();
    return;
  }

  const int kg = w >> 2, mh = (w >> 1) & 1, nh = (w & 1) ^ kg;
  const int g = lane >> 4, c = lane & 15;
  const int hoff = kg * 4096 + mh * 2048 + lane * 16;
  const int woff = 8192 + kg * 5120 + (4 * g + (c >> 2)) * 160 + 8 * (c & 3) + nh * 96;
  f32x4 acc[3][2];
#pragma unroll
  for (int t = 0; t < 3; ++t)
#pragma unroll
    for (int i = 0; i < 2; ++i) acc[t][i] = f32x4{0.f, 0.f, 0.f, 0.f};

  bf16x8 ha[2], wa[3], hb[2], wb[3];
  auto ldfrag = [&](const char* sb, bf16x8(&hf)[2], bf16x8(&wf)[3]) {
#pragma unroll
    for (int i = 0; i < 2; ++i) hf[i] = *(const bf16x8*)(sb + hoff + i * 1024);
#pragma unroll
    for (int q = 0; q < 3; ++q) wf[q] = cat(tr_read(sb + woff + q * 32), tr_read(sb + woff + 2560 + q * 32));
  };
  auto mma = [&](bf16x8(&hf)[2], bf16x8(&wf)[3]) {
#pragma unroll
    for (int q = 0; q < 2; ++q)
#pragma unroll
      for (int i = 0; i < 2; ++i)
        acc[q][i] = __builtin_amdgcn_mfma_f32_16x16x32_bf16(wf[q], hf[i], acc[q][i], 0, 0, 0);
    if (nh == 0) {
#pragma unroll
      for (int i = 0; i < 2; ++i)
        acc[2][i] = __builtin_amdgcn_mfma_f32_16x16x32_bf16(wf[2], hf[i], acc[2][i], 0, 0, 0);
    }
  };
  BAR();
  STAMP(2);
  ldfrag(smem, ha, wa);
  SCHED_FENCE();
  int slot = 0;
#pragma unroll 1
  for (int t = 0; t < G2_NT - 1; t += 2) {
    slot = slot == G2_NS - 1 ? 0 : slot + 1;
    ldfrag(smem + slot * G2_SB, hb, wb);
    SCHED_FENCE();
    mma(ha, wa);
    SCHED_FENCE();
    BAR();
    slot = slot == G2_NS - 1 ? 0 : slot + 1;
    ldfrag(smem + slot * G2_SB, ha, wa);
    SCHED_FENCE();
    mma(hb, wb);
    SCHED_FENCE();
    BAR();
    if (t < 18) STAMP(3 + t / 2);
  }
  mma(ha, wa);

  STAMP(12);
  BAR();
  char* rp = smem + (((mh << 1) | nh) * 6) * 1024 + lane * 16;
  if (kg == 1) {
#pragma unroll
    for (int t = 0; t < 3; ++t)
#pragma unroll
      for (int i = 0; i < 2; ++i)
        if (t < 2 || nh == 0) *(f32x4*)(rp + (t * 2 + i) * 1024) = acc[t][i];
  }
  asm volatile("s_waitcnt lgkmcnt(0)" ::: "memory");
  BAR();
  if (kg == 0) {
#pragma unroll
    for (int t = 0; t < 3; ++t)
#pragma unroll
      for (int i = 0; i < 2; ++i)
        if (t < 2 || nh == 0) {
          f32x4 s = acc[t][i] + *(const f32x4*)(rp + (t * 2 + i) * 1024);
          u32x2 v;
          v[0] = pack2(s[0], s[1]);
          v[1] = pack2(s[2], s[3]);
          st8(out + (size_t)(64 * mt + 32 * mh + 16 * i + c) * D_ + nt * 80 + nh * 48 + 16 * t + 4 * g, v);
        }
  }
  STAMP_END();
}

extern "C" void kernel_launch(void* const* d_in, const int* in_sizes, int n_in, void* d_out, int out_size,
                              void* d_ws, size_t ws_size, hipStream_t stream) {
  const __hip_bfloat16* x = (const __hip_bfloat16*)d_in[0];
  const __hip_bfloat16* w1 = (const __hip_bfloat16*)d_in[1];
  const __hip_bfloat16* w2 = (const __hip_bfloat16*)d_in[2];
  __hip_bfloat16* out = (__hip_bfloat16*)d_out;
  char* ws = (char*)d_ws;
  (void)hipFuncSetAttribute((const void*)ffn_gemm1, hipFuncAttributeMaxDynamicSharedMemorySize, G1_LDS);
  (void)hipFuncSetAttribute((const void*)ffn_gemm2, hipFuncAttributeMaxDynamicSharedMemorySize, G2_LDS);
  ffn_gemm1<<<256, NTHR, G1_LDS, stream>>>(x, w1, w2, ws);
  ffn_gemm2<<<256, NTHR, G2_LDS, stream>>>(ws, out);
}
```

```cpp
#include <hip/hip_runtime.h>
#include <hip/hip_bf16.h>
#include <stdint.h>

typedef __attribute__((ext_vector_type(8))) short bf16x8;
typedef __attribute__((ext_vector_type(4))) short s16x4;
typedef __attribute__((ext_vector_type(4))) float f32x4;
typedef __attribute__((ext_vector_type(2))) unsigned u32x2;
typedef __attribute__((ext_vector_type(4))) unsigned u32x4;
#define LDS_AS __attribute__((address_space(3)))

constexpr int M_ = 2048, D_ = 640, F_ = 1728;
constexpr int NFB = F_ / 32;
constexpr size_t W2P_OFF = 8u << 20;
constexpr int NTHR = 768;

#define WAIT_V(n) asm volatile("s_waitcnt vmcnt(%0)" ::"i"(n) : "memory")
#define BAR()                        \
  do {                               \
    asm volatile("" ::: "memory");   \
    __builtin_amdgcn_s_barrier();    \
    asm volatile("" ::: "memory");   \
  } while (0)
#define SGB(mask, n) __builtin_amdgcn_sched_group_barrier(mask, n, 0)
#define SCHED_FENCE() __builtin_amdgcn_sched_barrier(0)

__device__ __forceinline__ void glds16(const void* sbase, unsigned voff, unsigned lds) {
  asm volatile("s_mov_b32 m0, %2\n\ts_nop 0\n\tglobal_load_lds_dwordx4 %0, %1" ::"v"(voff), "s"(sbase), "s"(lds));
}
__device__ __forceinline__ void st16_wt(void* p, u32x4 v) {
  asm volatile("global_store_dwordx4 %0, %1, off sc1\n\ts_nop 1" ::"v"(p), "v"(v) : "memory");
}
__device__ __forceinline__ unsigned lds_addr(const void* p) { return (unsigned)(uintptr_t)(LDS_AS const char*)p; }
__device__ __forceinline__ s16x4 tr_read(const char* p) {
  return __builtin_amdgcn_ds_read_tr16_b64_v4i16((LDS_AS s16x4*)p);
}
__device__ __forceinline__ bf16x8 cat(s16x4 lo, s16x4 hi) {
  return __builtin_shufflevector(lo, hi, 0, 1, 2, 3, 4, 5, 6, 7);
}
__device__ __forceinline__ unsigned pack2(float a, float b) {
  __hip_bfloat16 x = __float2bfloat16(a), y = __float2bfloat16(b);
  return (unsigned)__builtin_bit_cast(unsigned short, x) | ((unsigned)__builtin_bit_cast(unsigned short, y) << 16);
}
__device__ __forceinline__ unsigned silu2(float a, float b) {
  unsigned p = pack2(a, b);
  float ha = __builtin_bit_cast(float, p << 16), hb = __builtin_bit_cast(float, p & 0xffff0000u);
  float ea = __builtin_amdgcn_exp2f(ha * -1.4426950408889634f), eb = __builtin_amdgcn_exp2f(hb * -1.4426950408889634f);
  float ra = __builtin_amdgcn_rcpf(1.0f + ea), rb = __builtin_amdgcn_rcpf(1.0f + eb);
  return pack2(ha * ra, hb * rb);
}

constexpr int G1_NS = 4;
constexpr int G1_XB = 128 * 128;
constexpr int G1_WB = 64 * 256;
constexpr int G1_SB = G1_XB + G1_WB;
constexpr int G1_LDS = G1_NS * G1_SB;
constexpr int G1_NT = D_ / 64;

__global__ __launch_bounds__(NTHR, 3) void ffn_gemm1(const __hip_bfloat16* __restrict__ x,
                                                     const __hip_bfloat16* __restrict__ w1,
                                                     const __hip_bfloat16* __restrict__ w2,
                                                     char* __restrict__ ws) {
  extern __shared__ __attribute__((aligned(16))) char smem[];
  const int bid = blockIdx.x;
  const int xcd = bid & 7, idx = bid >> 3;
  const int tid = threadIdx.x;

  if (idx >= 28) {
    if (tid >= 512) return;
    const int gt = ((idx - 28) * 8 + xcd) * 512 + tid;
    char* dst = ws + W2P_OFF;
    constexpr int NCH = F_ * D_ / 8;
    u32x4 v[9];
#pragma unroll
    for (int r = 0; r < 9; ++r) {
      int ch = gt + r * 16384;
      if (ch < NCH) {
        int blk = ch / 320, o = ch - blk * 320;
        int nt = blk / NFB, b = blk - nt * NFB;
        int row = o / 10, cc = o - row * 10;
        v[r] = *(const u32x4*)(w2 + (size_t)(32 * b + row) * D_ + nt * 80 + cc * 8);
      }
    }
#pragma unroll
    for (int r = 0; r < 9; ++r) {
      int ch = gt + r * 16384;
      if (ch < NCH) st16_wt(dst + (size_t)ch * 16, v[r]);
    }
    return;
  }

  const int mt = 4 * (xcd >> 1) + idx / 7;
  const int nt = 7 * (xcd & 1) + idx % 7;
  const int m0 = mt * 128, n0 = nt * 128;
  const int w = __builtin_amdgcn_readfirstlane(tid >> 6);
  const int lane = tid & 63;

  if (w >= 8) {
    const int l = w - 8;
    const unsigned lbase = lds_addr(smem);
    unsigned xsrc[4], wsrc[4];
#pragma unroll
    for (int q = 0; q < 4; ++q) {
      int row = 8 * (4 * l + q) + (lane >> 3);
      int ch = (lane & 7) ^ ((row >> 1) & 7);
      xsrc[q] = ((m0 + row) * D_ + ch * 8) * 2;
      int p = 4 * (4 * l + q) + (lane >> 4), pos = lane & 15;
      int r = (p & 0x33) | ((p & 4) << 1) | ((p & 8) >> 1);
      int wch = (((pos >> 1) ^ (p & 7)) << 1) | (pos & 1);
      int col = n0 + wch * 8;
      col = col > (F_ - 8) ? (F_ - 8) : col;
      wsrc[q] = (r * F_ + col) * 2;
    }
    auto stage = [&](int t, unsigned sb) {
#pragma unroll
      for (int q = 0; q < 4; ++q) {
        glds16((const char*)x + t * 128, xsrc[q], sb + (4 * l + q) * 1024);
        glds16((const char*)w1 + (size_t)t * 64 * F_ * 2, wsrc[q], sb + G1_XB + (4 * l + q) * 1024);
      }
    };
#pragma unroll
    for (int t = 0; t < G1_NS - 1; ++t) stage(t, lbase + t * G1_SB);
    int ps = G1_NS - 1;
#pragma unroll 1
    for (int t = 0; t < G1_NT - (G1_NS - 1); ++t) {
      WAIT_V(8 * (G1_NS - 3));
      BAR();
      stage(t + G1_NS - 1, lbase + ps * G1_SB);
      ps = ps == G1_NS - 1 ? 0 : ps + 1;
    }
#pragma unroll
    for (int r = G1_NS - 3; r >= -1; --r) {
      WAIT_V(8 * (r < 0 ? 0 : r));
      BAR();
    }
    return;
  }

  const int wm = w >> 1, wn = w & 1;
  const int g = lane >> 4, c = lane & 15;
  const int xoff0 = (32 * wm + c) * 128 + ((g ^ (c >> 1)) << 4);
  const int xoff1 = xoff0 ^ 64;
  int woff[4];
  {
    int p = 16 * (g >> 1) + 4 * (g & 1) + (c >> 2);
#pragma unroll
    for (int n = 0; n < 4; ++n) woff[n] = G1_XB + p * 256 + (((4 * wn + n) ^ (p & 7)) << 5) + 8 * (c & 3);
  }

  f32x4 acc[4][2];
#pragma unroll
  for (int n = 0; n < 4; ++n)
#pragma unroll
    for (int i = 0; i < 2; ++i) acc[n][i] = f32x4{0.f, 0.f, 0.f, 0.f};

  bf16x8 xa[2], wa[4], xb[2], wb[4];
  auto ldfrag = [&](const char* sb, int s, bf16x8(&xf)[2], bf16x8(&wf)[4]) {
#pragma unroll
    for (int i = 0; i < 2; ++i) xf[i] = *(const bf16x8*)(sb + (s ? xoff1 : xoff0) + i * 2048);
#pragma unroll
    for (int n = 0; n < 4; ++n)
      wf[n] = cat(tr_read(sb + woff[n] + s * 8192), tr_read(sb + woff[n] + s * 8192 + 2048));
  };
  auto mma = [&](bf16x8(&xf)[2], bf16x8(&wf)[4]) {
#pragma unroll
    for (int n = 0; n < 4; ++n)
#pragma unroll
      for (int i = 0; i < 2; ++i)
        acc[n][i] = __builtin_amdgcn_mfma_f32_16x16x32_bf16(wf[n], xf[i], acc[n][i], 0, 0, 0);
  };
  auto interleave = [&]() {
    SGB(0x100, 2);
#pragma unroll
    for (int j = 0; j < 8; ++j) { SGB(0x8, 1); SGB(0x100, 1); }
  };
  BAR();
  ldfrag(smem, 0, xa, wa);
  SCHED_FENCE();
  int slot = 0;
#pragma unroll 1
  for (int t = 0; t < G1_NT - 1; ++t) {
    const char* sb = smem + slot * G1_SB;
    slot = slot == G1_NS - 1 ? 0 : slot + 1;
    ldfrag(sb, 1, xb, wb);
    mma(xa, wa);
    interleave();
    SCHED_FENCE();
    ldfrag(smem + slot * G1_SB, 0, xa, wa);
    mma(xb, wb);
    interleave();
    SCHED_FENCE();
    BAR();
  }
  ldfrag(smem + slot * G1_SB, 1, xb, wb);
  mma(xa, wa);
  interleave();
  SCHED_FENCE();
  mma(xb, wb);

#pragma unroll
  for (int j = 0; j < 2; ++j) {
    const int fb = 4 * nt + 2 * wn + j;
    if (fb < NFB) {
#pragma unroll
      for (int i = 0; i < 2; ++i) {
        const int m16 = 8 * mt + 2 * wm + i;
        u32x4 v;
        v[0] = silu2(acc[2 * j][i][0], acc[2 * j][i][1]);
        v[1] = silu2(acc[2 * j][i][2], acc[2 * j][i][3]);
        v[2] = silu2(acc[2 * j + 1][i][0], acc[2 * j + 1][i][1]);
        v[3] = silu2(acc[2 * j + 1][i][2], acc[2 * j + 1][i][3]);
        st16_wt(ws + ((size_t)(m16 * NFB + fb) * 64 + lane) * 16, v);
      }
    }
  }
}

constexpr int G2_NS = 8;
constexpr int G2_SB = 8192 + 10240;
constexpr int G2_LDS = G2_NS * G2_SB + 64;
constexpr int G2_NT = NFB / 2;

__global__ __launch_bounds__(NTHR, 3) void ffn_gemm2(const char* __restrict__ ws, __hip_bfloat16* __restrict__ out) {
  extern __shared__ __attribute__((aligned(16))) char smem[];
  const int bid = blockIdx.x;
  const int xcd = bid & 7, idx = bid >> 3;
  const int mt = 8 * (xcd >> 1) + (idx >> 2);
  const int nt = 4 * (xcd & 1) + (idx & 3);
  const int tid = threadIdx.x;
  const int w = __builtin_amdgcn_readfirstlane(tid >> 6);
  const int lane = tid & 63;

  if (w >= 8) {
    const int l = w - 8;
    const unsigned lbase = lds_addr(smem);
    const unsigned loff = lane * 16;
    const char* hbase = ws + (size_t)((4 * mt + 2 * (l & 1)) * NFB + (l >> 1)) * 1024;
    const char* wbase = ws + W2P_OFF + (size_t)nt * NFB * 5120 + l * 1024;
    const unsigned hl = (l >> 1) * 4096 + (l & 1) * 2048;
    auto stage = [&](int t, unsigned sb) {
      glds16(hbase + t * 2048, loff, sb + hl);
      glds16(wbase + t * 10240, loff, sb + 8192 + l * 1024);
      glds16(hbase + t * 2048 + NFB * 1024, loff, sb + hl + 1024);
      glds16(wbase + t * 10240 + 4096, loff, sb + 8192 + 4096 + l * 1024);
      if (l < 2) glds16(wbase + t * 10240 + 8192, loff, sb + 8192 + 8192 + l * 1024);
    };
#pragma unroll
    for (int t = 0; t < G2_NS - 1; ++t) stage(t, lbase + t * G2_SB);
    int ps = G2_NS - 1;
#pragma unroll 1
    for (int t = 0; t < G2_NT - (G2_NS - 1); ++t) {
      if (l < 2) { WAIT_V(5 * (G2_NS - 3)); } else { WAIT_V(4 * (G2_NS - 3)); }
      BAR();
      stage(t + G2_NS - 1, lbase + ps * G2_SB);
      ps = ps == G2_NS - 1 ? 0 : ps + 1;
    }
#pragma unroll
    for (int r = G2_NS - 3; r >= -1; --r) {
      if (l < 2) { WAIT_V(5 * (r < 0 ? 0 : r)); } else { WAIT_V(4 * (r < 0 ? 0 : r)); }
      BAR();
    }
    BAR();
    return;
  }

  const int kg = w >> 2, mh = (w >> 1) & 1, nh = (w & 1) ^ kg;
  const int g = lane >> 4, c = lane & 15;
  const int hoff = kg * 4096 + mh * 2048 + lane * 16;
  const int woff = 8192 + kg * 5120 + (4 * g + (c >> 2)) * 160 + 8 * (c & 3) + nh * 96;
  f32x4 acc[3][2];
#pragma unroll
  for (int t = 0; t < 3; ++t)
#pragma unroll
    for (int i = 0; i < 2; ++i) acc[t][i] = f32x4{0.f, 0.f, 0.f, 0.f};
  bf16x8 ha[2], wa[3], hb[2], wb[3];
  auto ldfrag = [&](const char* sb, bf16x8(&hf)[2], bf16x8(&wf)[3]) {
#pragma unroll
    for (int i = 0; i < 2; ++i) hf[i] = *(const bf16x8*)(sb + hoff + i * 1024);
#pragma unroll
    for (int q = 0; q < 3; ++q) wf[q] = cat(tr_read(sb + woff + q * 32), tr_read(sb + woff + 2560 + q * 32));
  };
  auto mma = [&](bf16x8(&hf)[2], bf16x8(&wf)[3]) {
#pragma unroll
    for (int q = 0; q < 2; ++q)
#pragma unroll
      for (int i = 0; i < 2; ++i)
        acc[q][i] = __builtin_amdgcn_mfma_f32_16x16x32_bf16(wf[q], hf[i], acc[q][i], 0, 0, 0);
    if (nh == 0) {
#pragma unroll
      for (int i = 0; i < 2; ++i)
        acc[2][i] = __builtin_amdgcn_mfma_f32_16x16x32_bf16(wf[2], hf[i], acc[2][i], 0, 0, 0);
    }
  };
  BAR();
  ldfrag(smem, ha, wa);
  SCHED_FENCE();
  int slot = 0;
#pragma unroll 1
  for (int t = 0; t < G2_NT - 1; t += 2) {
    slot = slot == G2_NS - 1 ? 0 : slot + 1;
    ldfrag(smem + slot * G2_SB, hb, wb);
    SCHED_FENCE();
    mma(ha, wa);
    SCHED_FENCE();
    BAR();
    slot = slot == G2_NS - 1 ? 0 : slot + 1;
    ldfrag(smem + slot * G2_SB, ha, wa);
    SCHED_FENCE();
    mma(hb, wb);
    SCHED_FENCE();
    BAR();
  }
  mma(ha, wa);
  char* rp = smem + 3 * G2_SB + (((mh << 1) | nh) * 6) * 1024 + lane * 16;
  f32x4 keep[3], give[3];
#pragma unroll
  for (int t = 0; t < 3; ++t)
#pragma unroll
    for (int e = 0; e < 4; ++e) {
      keep[t][e] = kg ? acc[t][1][e] : acc[t][0][e];
      give[t][e] = kg ? acc[t][0][e] : acc[t][1][e];
    }
#pragma unroll
  for (int t = 0; t < 3; ++t)
    if (t < 2 || nh == 0) *(f32x4*)(rp + (t * 2 + kg) * 1024) = give[t];
  asm volatile("s_waitcnt lgkmcnt(0)" ::: "memory");
  BAR();
#pragma unroll
  for (int t = 0; t < 3; ++t)
    if (t < 2 || nh == 0) {
      f32x4 s = keep[t] + *(const f32x4*)(rp + (t * 2 + (kg ^ 1)) * 1024);
      u32x2 v;
      v[0] = pack2(s[0], s[1]);
      v[1] = pack2(s[2], s[3]);
      *(u32x2*)(out + (size_t)(64 * mt + 32 * mh + 16 * kg + c) * D_ + nt * 80 + nh * 48 + 16 * t + 4 * g) = v;
    }
}

extern "C" void kernel_launch(void* const* d_in, const int* in_sizes, int n_in, void* d_out, int out_size,
                              void* d_ws, size_t ws_size, hipStream_t stream) {
  const __hip_bfloat16* x = (const __hip_bfloat16*)d_in[0];
  const __hip_bfloat16* w1 = (const __hip_bfloat16*)d_in[1];
  const __hip_bfloat16* w2 = (const __hip_bfloat16*)d_in[2];
  __hip_bfloat16* out = (__hip_bfloat16*)d_out;
  char* ws = (char*)d_ws;
  (void)hipFuncSetAttribute((const void*)ffn_gemm1, hipFuncAttributeMaxDynamicSharedMemorySize, G1_LDS);
  (void)hipFuncSetAttribute((const void*)ffn_gemm2, hipFuncAttributeMaxDynamicSharedMemorySize, G2_LDS);
  ffn_gemm1<<<256, NTHR, G1_LDS, stream>>>(x, w1, w2, ws);
  ffn_gemm2<<<256, NTHR, G2_LDS, stream>>>(ws, out);
}
```
